```python
import torch

def _softmax(x, axis=-1):
    return torch.softmax(x, dim=axis)
def _gelu(x):
    return torch.nn.functional.gelu(x, approximate="tanh")
def _silu(x):
    return torch.nn.functional.silu(x)
def _sigmoid(x):
    return torch.sigmoid(x)
def _layernorm(x, axis=-1, eps=1e-5):
    m = x.mean(dim=axis, keepdim=True)
    v = x.var(dim=axis, keepdim=True, unbiased=False)
    return (x - m) / torch.sqrt(v + eps)
def _scatter_add(tgt, idx, src, axis=0):
    return tgt.clone().index_add_(axis, idx, src)
def _segment_sum(src, seg, n):
    out = torch.zeros((n,) + src.shape[1:], dtype=src.dtype)
    return out.index_add_(0, seg, src)
def _pad(x, pw):
    flat = tuple(v for pair in reversed(pw) for v in pair)
    return torch.nn.functional.pad(x, flat)

kernel_name = 'standalone__groupnorm__sampling'

def setup_inputs():

    _w = (lambda a: (lambda t: t.to(torch.bfloat16) if t.is_floating_point() else t)(torch.as_tensor(a)))
    B = 32
    G = 32
    cpg = max(1, 512 // G)
    C = G * cpg
    H, W = 32, 16
    return {
        "x": _w(abs((lambda *s: torch.randn(*s, dtype=torch.bfloat16) * 0.05)(B, C, H, W))),
        "g": _w((lambda *s: torch.randn(*s, dtype=torch.bfloat16) * 0.05)(C) + 1.0),
        "b": _w((lambda *s: torch.randn(*s, dtype=torch.bfloat16) * 0.4)(C)),
    }

def reference(x, g, b):
    G = 32
    B, C, H, W = x.shape[0], x.shape[1], x.shape[2], x.shape[3]
    xr = x.reshape(B, G, C // G, H, W)
    mu = xr.mean(axis=(2, 3, 4), keepdims=True)
    var = ((xr - mu) ** 2).mean(axis=(2, 3, 4), keepdims=True)
    xn = (xr - mu) / torch.sqrt(var + 1e-5)
    xn = xn.reshape(B, C, H, W)
    return xn * g[None, :, None, None] + b[None, :, None, None]
```

```cpp
#include <hip/hip_runtime.h>
#include <stdint.h>

typedef unsigned int u32;
typedef __attribute__((ext_vector_type(4))) unsigned int u32x4;
typedef __attribute__((ext_vector_type(2))) unsigned int u32x2;
typedef __attribute__((ext_vector_type(2))) float f32x2;
typedef __attribute__((ext_vector_type(4))) float f32x4;
typedef __attribute__((ext_vector_type(2))) __bf16 bf16x2;

constexpr int kGroups = 32;
constexpr int kCPG = 16;
constexpr int kHW = 512;
constexpr int kGroupElems = kCPG * kHW;
constexpr int kGroupBytes = kGroupElems * 2;
constexpr float kEps = 1e-5f;

constexpr int kAuxLoad = 2;
constexpr int kAuxStore = 18;
constexpr u32 kGateTicks = 240;
constexpr u32 kGateStep = 40;

__device__ __forceinline__ float bf_lo(u32 w) { return __uint_as_float(w << 16); }
__device__ __forceinline__ float bf_hi(u32 w) { return __uint_as_float(w & 0xffff0000u); }
__device__ __forceinline__ u32 pack_bf16(f32x2 f) {
  return __builtin_bit_cast(u32, __builtin_convertvector(f, bf16x2));
}
template <int CTRL>
__device__ __forceinline__ float dpp_add(float v) {
  return v + __int_as_float(__builtin_amdgcn_update_dpp(0, __float_as_int(v), CTRL, 0xf, 0xf, true));
}

__global__ __launch_bounds__(256) void groupnorm_kernel(const char* __restrict__ x, const u32* __restrict__ gamma,
                                                        const u32* __restrict__ beta, char* __restrict__ y) {
  const u32 t_start = (u32)__builtin_amdgcn_s_memrealtime();
  const u32 tid = threadIdx.x;
  const u32 grp = blockIdx.x;
  __shared__ f32x4 red[4];

  u32 voff = ((tid & 0xC0u) << 6) | ((tid & 63u) << 4);
  asm volatile("" : "+v"(voff));
  const auto rx =
      __builtin_amdgcn_make_buffer_rsrc((void*)(x + (size_t)grp * kGroupBytes), 0, kGroupBytes, 0x00020000);
  u32x4 v[4];
#pragma unroll
  for (int j = 0; j < 4; ++j) v[j] = __builtin_amdgcn_raw_buffer_load_b128(rx, voff + j * 1024, 0, kAuxLoad);
  __builtin_amdgcn_sched_barrier(0);

  const u32 wave = __builtin_amdgcn_readfirstlane(tid >> 6);
  const u32 cidx = (grp & (kGroups - 1)) * (kCPG / 2) + wave * 2;
  u32x2 gw = *(const u32x2*)(gamma + cidx);
  u32x2 bw = *(const u32x2*)(beta + cidx);
  u32 sel_lo = 0x00003f80u, sel_hi = 0x3f800000u;
  auto lds_wr = (__attribute__((address_space(3))) float*)&((float*)red)[(tid >> 6) * 4 + ((tid >> 5) & 1)];
  auto lds_rd = (__attribute__((address_space(3))) f32x4*)&red[tid & 3];
  asm volatile("" : "+v"(sel_lo), "+v"(sel_hi), "+v"(lds_wr), "+v"(lds_rd));
  __builtin_amdgcn_sched_barrier(0);

  const u32 kbits = __builtin_amdgcn_readfirstlane(v[0][0]) << 16;
  const float negK = __uint_as_float(kbits ^ 0x80000000u);
  f32x2 d[16];
  f32x2 s1, s2;
#pragma unroll
  for (int j = 0; j < 4; ++j) {
    float e0, e1, e2, e3, e4, e5, e6, e7;
    asm volatile(
        "v_dot2_f32_bf16 %0, %8, %12, %14\n\t"
        "v_dot2_f32_bf16 %1, %8, %13, %14\n\t"
        "v_dot2_f32_bf16 %2, %9, %12, %14\n\t"
        "v_dot2_f32_bf16 %3, %9, %13, %14\n\t"
        "v_dot2_f32_bf16 %4, %10, %12, %14\n\t"
        "v_dot2_f32_bf16 %5, %10, %13, %14\n\t"
        "v_dot2_f32_bf16 %6, %11, %12, %14\n\t"
        "v_dot2_f32_bf16 %7, %11, %13, %14"
        : "=&v"(e0), "=&v"(e1), "=&v"(e2), "=&v"(e3), "=&v"(e4), "=&v"(e5), "=&v"(e6), "=&v"(e7)
        : "v"(v[j][0]), "v"(v[j][1]), "v"(v[j][2]), "v"(v[j][3]), "v"(sel_lo), "v"(sel_hi), "s"(negK));
    d[j * 4 + 0] = f32x2{e0, e1};
    d[j * 4 + 1] = f32x2{e2, e3};
    d[j * 4 + 2] = f32x2{e4, e5};
    d[j * 4 + 3] = f32x2{e6, e7};
    if (j == 0) {
      asm volatile(
          "v_pk_mul_f32 %1, %2, %2\n\t"
          "v_pk_add_f32 %0, %2, %3\n\t"
          "v_pk_fma_f32 %1, %3, %3, %1\n\t"
          "v_pk_add_f32 %0, %0, %4\n\t"
          "v_pk_fma_f32 %1, %4, %4, %1\n\t"
          "v_pk_add_f32 %0, %0, %5\n\t"
          "v_pk_fma_f32 %1, %5, %5, %1"
          : "=&v"(s1), "=&v"(s2)
          : "v"(d[0]), "v"(d[1]), "v"(d[2]), "v"(d[3]));
    } else {
      asm volatile(
          "v_pk_add_f32 %0, %0, %2\n\t"
          "v_pk_fma_f32 %1, %2, %2, %1\n\t"
          "v_pk_add_f32 %0, %0, %3\n\t"
          "v_pk_fma_f32 %1, %3, %3, %1\n\t"
          "v_pk_add_f32 %0, %0, %4\n\t"
          "v_pk_fma_f32 %1, %4, %4, %1\n\t"
          "v_pk_add_f32 %0, %0, %5\n\t"
          "v_pk_fma_f32 %1, %5, %5, %1"
          : "+v"(s1), "+v"(s2)
          : "v"(d[j * 4 + 0]), "v"(d[j * 4 + 1]), "v"(d[j * 4 + 2]), "v"(d[j * 4 + 3]));
    }
  }

  float a1 = s1[0] + s1[1], a2 = s2[0] + s2[1];
  const float kv = __uint_as_float(kbits);
  float r;
  f32x4 p;
  unsigned long long t_bar;
  asm volatile(
      "s_nop 1\n\t"
      "v_permlane32_swap_b32 %1, %2\n\t"
      "s_nop 0\n\t"
      "v_add_f32 %0, %1, %2\n\t"
      "s_nop 1\n\t"
      "v_add_f32_dpp %0, %0, %0 row_shr:1 row_mask:0xf bank_mask:0xf\n\t"
      "s_nop 1\n\t"
      "v_add_f32_dpp %0, %0, %0 row_shr:2 row_mask:0xf bank_mask:0xf\n\t"
      "s_nop 1\n\t"
      "v_add_f32_dpp %0, %0, %0 row_shr:4 row_mask:0xf bank_mask:0xf\n\t"
      "s_nop 1\n\t"
      "v_add_f32_dpp %0, %0, %0 row_shr:8 row_mask:0xf bank_mask:0xf\n\t"
      "s_nop 1\n\t"
      "v_add_f32_dpp %0, %0, %0 row_bcast:15 row_mask:0xa bank_mask:0xf\n\t"
      "s_brev_b32 exec_lo, 1\n\t"
      "s_brev_b32 exec_hi, 1\n\t"
      "ds_write2_b32 %5, %0, %6 offset0:0 offset1:2\n\t"
      "s_mov_b64 exec, -1\n\t"
      "s_waitcnt lgkmcnt(0)\n\t"
      "s_barrier\n\t"
      "ds_read_b128 %3, %7\n\t"
      "s_memrealtime %4\n\t"
      "s_waitcnt lgkmcnt(0)"
      : "=&v"(r), "+v"(a1), "+v"(a2), "=&v"(p), "=&s"(t_bar)
      : "v"(lds_wr), "v"(kv), "v"(lds_rd)
      : "memory");
  u32 t_now = (u32)t_bar;
  asm volatile("" : "+s"(gw), "+s"(bw));

  constexpr float nw = kGroupElems / 4;
  const float dk = p[2] - kv;
  float t1 = fmaf(nw, dk, p[0]);
  float t2 = fmaf(dk, t1 + p[0], p[1]);
  t1 = dpp_add<0xB1>(t1);
  t2 = dpp_add<0xB1>(t2);
  t1 = dpp_add<0x4E>(t1);
  t2 = dpp_add<0x4E>(t2);
  constexpr float inv_n = 1.0f / kGroupElems;
  const float m = t1 * inv_n;
  const float ve = fmaxf(fmaf(-m, m, fmaf(t2, inv_n, kEps)), kEps);
  float rstd = __builtin_amdgcn_rsqf(ve);
  const auto ry =
      __builtin_amdgcn_make_buffer_rsrc((void*)(y + (size_t)grp * kGroupBytes), 0, kGroupBytes, 0x00020000);

  asm volatile("" : "+v"(rstd));
  const u32 gate = grp < 1024u ? kGateTicks + (grp >> 8) * kGateStep : 0u;
  for (int it = 0; t_now - t_start < gate && it < 256; ++it) {
    __builtin_amdgcn_s_sleep(1);
    t_now = (u32)__builtin_amdgcn_s_memrealtime();
  }

  const float ga[4] = {bf_lo(gw[0]), bf_hi(gw[0]), bf_lo(gw[1]), bf_hi(gw[1])};
  const float be[4] = {bf_lo(bw[0]), bf_hi(bw[0]), bf_lo(bw[1]), bf_hi(bw[1])};
#pragma unroll
  for (int j = 0; j < 4; ++j) {
    const float a = rstd * ga[j];
    const float c = fmaf(-m, a, be[j]);
    const f32x2 a2v = {a, a}, c2v = {c, c};
    u32x4 o;
#pragma unroll
    for (int k = 0; k < 4; ++k) o[k] = pack_bf16(__builtin_elementwise_fma(d[j * 4 + k], a2v, c2v));
    __builtin_amdgcn_raw_buffer_store_b128(o, ry, voff + j * 1024, 0, kAuxStore);
    __builtin_amdgcn_sched_barrier(0);
  }
}

extern "C" void kernel_launch(void* const* d_in, const int* in_sizes, int n_in, void* d_out, int out_size, void* d_ws,
                              size_t ws_size, hipStream_t stream) {
  const int ngroups = in_sizes[0] / kGroupElems;
  groupnorm_kernel<<<ngroups, 256, 0, stream>>>((const char*)d_in[0], (const u32*)d_in[1], (const u32*)d_in[2],
                                                (char*)d_out);
}
```
